```python
import torch

def _softmax(x, axis=-1):
    return torch.softmax(x, dim=axis)
def _gelu(x):
    return torch.nn.functional.gelu(x, approximate="tanh")
def _silu(x):
    return torch.nn.functional.silu(x)
def _sigmoid(x):
    return torch.sigmoid(x)
def _layernorm(x, axis=-1, eps=1e-5):
    m = x.mean(dim=axis, keepdim=True)
    v = x.var(dim=axis, keepdim=True, unbiased=False)
    return (x - m) / torch.sqrt(v + eps)
def _scatter_add(tgt, idx, src, axis=0):
    return tgt.clone().index_add_(axis, idx, src)
def _segment_sum(src, seg, n):
    out = torch.zeros((n,) + src.shape[1:], dtype=src.dtype)
    return out.index_add_(0, seg, src)
def _pad(x, pw):
    flat = tuple(v for pair in reversed(pw) for v in pair)
    return torch.nn.functional.pad(x, flat)

kernel_name = 'recurrent_scan__sliding_block_cached__swiglu_ffn__decode'

def setup_inputs():

    _w = (lambda a: (lambda t: t.to(torch.bfloat16) if t.is_floating_point() else t)(torch.as_tensor(a)))
    def _A_setup():
        B, Sq, D = 1, 1, 576
        Hq, Hkv, Sk, Dh = 32, 16, 8192, 128
        assert Hq % Hkv == 0, (Hq, Hkv)
        return {
            "x": _w((lambda *s: torch.randn(*s, dtype=torch.bfloat16) * 0.05)(B, Sq, D)),
            "k_cache": _w((lambda *s: torch.randn(*s, dtype=torch.bfloat16) * 0.8)(B, Hkv, Sk, Dh)),
            "v_cache": _w((lambda *s: torch.randn(*s, dtype=torch.bfloat16) * 0.2)(B, Hkv, Sk, Dh)),
            "wq": _w((lambda *s: torch.randn(*s, dtype=torch.bfloat16) * 0.8)(D, Hq * Dh) * (D ** -0.5)),
            "wo": _w((lambda *s: torch.randn(*s, dtype=torch.bfloat16) * 0.2)(Hq * Dh, D) * ((Hq * Dh) ** -0.5)),
        }
    _A_in = _A_setup()

    def _B_setup():
        B, S, D, F = 1, 1, 576, 4608
        return {
            "x": _w((lambda *s: torch.randn(*s, dtype=torch.bfloat16) * 0.05)(B, S, D)),
            "w_gate": _w((lambda *s: torch.randn(*s, dtype=torch.bfloat16) * 0.1)(D, F)),
            "w_up": _w((lambda *s: torch.randn(*s, dtype=torch.bfloat16) * 0.1)(D, F)),
            "w_down": _w((lambda *s: torch.randn(*s, dtype=torch.bfloat16) * 0.1)(F, D)),
        }
    _B_in = _B_setup()

    return {
        "A_x": _A_in["x"],
        "A_k_cache": _A_in["k_cache"],
        "A_v_cache": _A_in["v_cache"],
        "A_wq": _A_in["wq"],
        "A_wo": _A_in["wo"],
        "B_x": _B_in["x"],
        "B_w_gate": _B_in["w_gate"],
        "B_w_up": _B_in["w_up"],
        "B_w_down": _B_in["w_down"],
    }

def reference(A_x, A_k_cache, A_v_cache, A_wq, A_wo, B_x, B_w_gate, B_w_up, B_w_down):
    def _A_ref(x, k_cache, v_cache, wq, wo):
        B, Sq, D = x.shape
        Hq, Dh = 32, 128
        Hkv, Sk = k_cache.shape[1], k_cache.shape[2]
        W = min(4096, max(256, Sk // 4))
        rep = Hq // Hkv
        q = (lambda x, *d: x.permute(*d))((x @ wq).reshape(B, Sq, Hq, Dh), 0, 2, 1, 3)
        k = (lambda x, n, axis: torch.repeat_interleave(x, n, dim=axis))(k_cache[:, :, Sk - W:, :], rep, axis=1)
        v = (lambda x, n, axis: torch.repeat_interleave(x, n, dim=axis))(v_cache[:, :, Sk - W:, :], rep, axis=1)
        scores = torch.einsum("bhqd,bhkd->bhqk", q, k) * (Dh ** -0.5)
        p = _softmax(scores, axis=-1)
        o = torch.einsum("bhqk,bhkd->bhqd", p, v)
        return (lambda x, *d: x.permute(*d))(o, 0, 2, 1, 3).reshape(B, Sq, Hq * Dh) @ wo

    def _B_ref(x, w_gate, w_up, w_down):
        g = _silu(x @ w_gate)
        u = x @ w_up
        return (g * u) @ w_down

    x = A_x
    xn = _layernorm(x, axis=-1)
    s = _A_ref(xn, A_k_cache, A_v_cache, A_wq, A_wo)
    g = _silu(_B_ref(xn, B_w_gate, B_w_up, B_w_down))
    return x + s * g
```

```cpp
#include <hip/hip_runtime.h>
#include <hip/hip_bf16.h>
#include <stdint.h>

typedef unsigned int u32;
typedef u32 u32x4 __attribute__((ext_vector_type(4)));

constexpr int DM = 576, HQ = 32, HKV = 16, SK = 8192, DH = 128, WIN = 2048, FF = 4608;
constexpr int NWG = 256;
constexpr int NT = 512;

__device__ __forceinline__ float bflo(u32 u) { return __uint_as_float(u << 16); }
__device__ __forceinline__ float bfhi(u32 u) { return __uint_as_float(u & 0xffff0000u); }
__device__ __forceinline__ float rbf(float f) { return __bfloat162float(__float2bfloat16(f)); }
__device__ __forceinline__ float silu_f(float g) { return g / (1.0f + __expf(-g)); }

__device__ __forceinline__ u32x4 ldnt16(const void* p) {
    return __builtin_nontemporal_load(reinterpret_cast<const u32x4*>(p));
}

__device__ __forceinline__ float wave_sum(float v) {
    v += __shfl_xor(v, 1); v += __shfl_xor(v, 2); v += __shfl_xor(v, 4);
    v += __shfl_xor(v, 8); v += __shfl_xor(v, 16); v += __shfl_xor(v, 32);
    return v;
}

__device__ __forceinline__ void layernorm_to_lds(const __hip_bfloat16* __restrict__ x, float* xn_lds, int lane) {
    const u32x4* xp = reinterpret_cast<const u32x4*>(x);
    u32x4 a = xp[lane];
    u32x4 b = {0u, 0u, 0u, 0u};
    if (lane < 8) b = xp[64 + lane];
    float va[8], vb[8];
#pragma unroll
    for (int j = 0; j < 4; ++j) { va[2 * j] = bflo(a[j]); va[2 * j + 1] = bfhi(a[j]); vb[2 * j] = bflo(b[j]); vb[2 * j + 1] = bfhi(b[j]); }
    float s = 0.f;
#pragma unroll
    for (int j = 0; j < 8; ++j) s += va[j];
    if (lane < 8) {
#pragma unroll
        for (int j = 0; j < 8; ++j) s += vb[j];
    }
    s = wave_sum(s);
    const float mean = s * (1.0f / DM);
    float q = 0.f;
#pragma unroll
    for (int j = 0; j < 8; ++j) { float d = va[j] - mean; q += d * d; }
    if (lane < 8) {
#pragma unroll
        for (int j = 0; j < 8; ++j) { float d = vb[j] - mean; q += d * d; }
    }
    q = wave_sum(q);
    const float var = rbf(q * (1.0f / DM));
    const float m = rbf(mean);
    const float den = rbf(sqrtf(rbf(var + 1e-5f)));
#pragma unroll
    for (int j = 0; j < 8; ++j) xn_lds[8 * lane + j] = rbf(rbf(va[j] - m) / den);
    if (lane < 8) {
#pragma unroll
        for (int j = 0; j < 8; ++j) xn_lds[512 + 8 * lane + j] = rbf(rbf(vb[j] - m) / den);
    }
}

struct Ws {
    float* qpart;
    float* gupart;
    float* apart;
    float* fpart;
    float* spart;
};

__global__ void __launch_bounds__(NT) k1(const __hip_bfloat16* __restrict__ x, const __hip_bfloat16* __restrict__ wq,
                                         const __hip_bfloat16* __restrict__ wg, const __hip_bfloat16* __restrict__ wu, Ws ws) {
    __shared__ float xn[DM];
    __shared__ float qp[8][64];
    __shared__ float Pg[7][DM];
    __shared__ float Pu[7][DM];
    const int t = threadIdx.x, lane = t & 63, wave = t >> 6, w = blockIdx.x;

    const int qcb = w >> 2, qks = w & 3;
    const int cc = t & 7, r0 = t >> 3;
    u32x4 wqv[3];
#pragma unroll
    for (int i = 0; i < 3; ++i) {
        int r = r0 + 64 * i;
        if (r < 144) wqv[i] = ldnt16(wq + (size_t)(144 * qks + r) * 4096 + 64 * qcb + 8 * cc);
        else wqv[i] = u32x4{0u, 0u, 0u, 0u};
    }
    const int gcb = w >> 5, gks = w & 31;
    const int gcc = t % 72, grg = t / 72;
    u32x4 gv[3], uv[3];
#pragma unroll
    for (int j = 0; j < 3; ++j) {
        int r = grg + 7 * j;
        bool ok = (t < 504) && (r < 18);
        size_t off = (size_t)(18 * gks + (ok ? r : 0)) * FF + 576 * gcb + 8 * (ok ? gcc : 0);
        gv[j] = ok ? ldnt16(wg + off) : u32x4{0u, 0u, 0u, 0u};
        uv[j] = ok ? ldnt16(wu + off) : u32x4{0u, 0u, 0u, 0u};
    }

    layernorm_to_lds(x, xn, lane);
    __syncthreads();

    float acc[8];
#pragma unroll
    for (int j = 0; j < 8; ++j) acc[j] = 0.f;
#pragma unroll
    for (int i = 0; i < 3; ++i) {
        int r = r0 + 64 * i;
        float xv = (r < 144) ? xn[144 * qks + r] : 0.f;
#pragma unroll
        for (int j = 0; j < 4; ++j) {
            acc[2 * j] = fmaf(xv, bflo(wqv[i][j]), acc[2 * j]);
            acc[2 * j + 1] = fmaf(xv, bfhi(wqv[i][j]), acc[2 * j + 1]);
        }
    }
#pragma unroll
    for (int j = 0; j < 8; ++j) {
        acc[j] += __shfl_xor(acc[j], 8);
        acc[j] += __shfl_xor(acc[j], 16);
        acc[j] += __shfl_xor(acc[j], 32);
    }
    if (lane < 8) {
#pragma unroll
        for (int j = 0; j < 8; ++j) qp[wave][8 * lane + j] = acc[j];
    }
    float ag[8], au[8];
#pragma unroll
    for (int j = 0; j < 8; ++j) { ag[j] = 0.f; au[j] = 0.f; }
#pragma unroll
    for (int j = 0; j < 3; ++j) {
        int r = grg + 7 * j;
        bool ok = (t < 504) && (r < 18);
        float xv = ok ? xn[18 * gks + r] : 0.f;
#pragma unroll
        for (int e = 0; e < 4; ++e) {
            ag[2 * e] = fmaf(xv, bflo(gv[j][e]), ag[2 * e]);
            ag[2 * e + 1] = fmaf(xv, bfhi(gv[j][e]), ag[2 * e + 1]);
            au[2 * e] = fmaf(xv, bflo(uv[j][e]), au[2 * e]);
            au[2 * e + 1] = fmaf(xv, bfhi(uv[j][e]), au[2 * e + 1]);
        }
    }
    if (t < 504) {
#pragma unroll
        for (int j = 0; j < 8; ++j) { Pg[grg][8 * gcc + j] = ag[j]; Pu[grg][8 * gcc + j] = au[j]; }
    }
    __syncthreads();
    if (t < 64) {
        float s = 0.f;
#pragma unroll
        for (int v = 0; v < 8; ++v) s += qp[v][t];
        ws.qpart[w * 64 + t] = s;
    }
    for (int n = t; n < DM; n += NT) {
        float sg = 0.f, su = 0.f;
#pragma unroll
        for (int v = 0; v < 7; ++v) { sg += Pg[v][n]; su += Pu[v][n]; }
        ws.gupart[((size_t)w * DM + n) * 2 + 0] = sg;
        ws.gupart[((size_t)w * DM + n) * 2 + 1] = su;
    }
}

__global__ void __launch_bounds__(NT) k2(const __hip_bfloat16* __restrict__ kc, const __hip_bfloat16* __restrict__ vc,
                                         const __hip_bfloat16* __restrict__ wd, Ws ws) {
    __shared__ float qs[2 * DH];
    __shared__ float hs[18];
    __shared__ float opart[8][2][DH];
    __shared__ float ml[8][2][2];
    __shared__ float Pf[7][DM];
    const int t = threadIdx.x, lane = t & 63, wave = t >> 6, w = blockIdx.x;
    const int g = w >> 4, s = w & 15;

    const size_t kvbase = ((size_t)g * SK + (SK - WIN) + 128 * s) * DH;
    u32x4 kv[4], vv[4];
#pragma unroll
    for (int i = 0; i < 4; ++i) {
        int key = 16 * wave + 4 * i + (lane >> 4);
        kv[i] = ldnt16(kc + kvbase + (size_t)key * DH + 8 * (lane & 15));
    }
#pragma unroll
    for (int i = 0; i < 4; ++i) {
        int key = 16 * wave + 4 * i + (lane >> 4);
        vv[i] = ldnt16(vc + kvbase + (size_t)key * DH + 8 * (lane & 15));
    }
    const int dcc = t % 72, drg = t / 72;
    u32x4 dv[3];
#pragma unroll
    for (int j = 0; j < 3; ++j) {
        int r = drg + 7 * j;
        bool ok = (t < 504) && (r < 18);
        dv[j] = ok ? ldnt16(wd + (size_t)(18 * w + r) * DM + 8 * dcc) : u32x4{0u, 0u, 0u, 0u};
    }
    if (t < 256) {
        int cb = t >> 6, c = t & 63;
        float sum = 0.f;
#pragma unroll
        for (int ks = 0; ks < 4; ++ks) sum += ws.qpart[(size_t)(16 * g + 4 * cb + ks) * 64 + c];
        qs[t] = rbf(sum);
    }
    if (t >= 256 && t < 256 + 36) {
        int j = (t - 256) % 18, which = (t - 256) / 18;
        int cb = w >> 5;
        float sum = 0.f;
        for (int ks = 0; ks < 32; ++ks) sum += ws.gupart[((size_t)(32 * cb + ks) * DM + (18 * w - 576 * cb + j)) * 2 + which];
        Pf[0][which * 18 + j] = rbf(sum);
    }
    __syncthreads();
    if (t < 18) {
        float gg = Pf[0][t], uu = Pf[0][18 + t];
        hs[t] = rbf(rbf(silu_f(gg)) * uu);
    }
    __syncthreads();

    float q0[8], q1[8];
#pragma unroll
    for (int j = 0; j < 8; ++j) { q0[j] = qs[8 * (lane & 15) + j]; q1[j] = qs[DH + 8 * (lane & 15) + j]; }
    float sc0[4], sc1[4];
#pragma unroll
    for (int i = 0; i < 4; ++i) {
        float a0 = 0.f, a1 = 0.f;
#pragma unroll
        for (int j = 0; j < 4; ++j) {
            float lo = bflo(kv[i][j]), hi = bfhi(kv[i][j]);
            a0 = fmaf(q0[2 * j], lo, a0); a0 = fmaf(q0[2 * j + 1], hi, a0);
            a1 = fmaf(q1[2 * j], lo, a1); a1 = fmaf(q1[2 * j + 1], hi, a1);
        }
        a0 += __shfl_xor(a0, 1); a1 += __shfl_xor(a1, 1);
        a0 += __shfl_xor(a0, 2); a1 += __shfl_xor(a1, 2);
        a0 += __shfl_xor(a0, 4); a1 += __shfl_xor(a1, 4);
        a0 += __shfl_xor(a0, 8); a1 += __shfl_xor(a1, 8);
        sc0[i] = rbf(rbf(a0) * 0.08838834764831845f);
        sc1[i] = rbf(rbf(a1) * 0.08838834764831845f);
    }
    float m0 = fmaxf(fmaxf(sc0[0], sc0[1]), fmaxf(sc0[2], sc0[3]));
    float m1 = fmaxf(fmaxf(sc1[0], sc1[1]), fmaxf(sc1[2], sc1[3]));
    m0 = fmaxf(m0, __shfl_xor(m0, 16)); m0 = fmaxf(m0, __shfl_xor(m0, 32));
    m1 = fmaxf(m1, __shfl_xor(m1, 16)); m1 = fmaxf(m1, __shfl_xor(m1, 32));
    float p0[4], p1[4], l0 = 0.f, l1 = 0.f;
#pragma unroll
    for (int i = 0; i < 4; ++i) { p0[i] = __expf(sc0[i] - m0); p1[i] = __expf(sc1[i] - m1); l0 += p0[i]; l1 += p1[i]; }
    l0 += __shfl_xor(l0, 16); l0 += __shfl_xor(l0, 32);
    l1 += __shfl_xor(l1, 16); l1 += __shfl_xor(l1, 32);
    float o0[8], o1[8];
#pragma unroll
    for (int j = 0; j < 8; ++j) { o0[j] = 0.f; o1[j] = 0.f; }
#pragma unroll
    for (int i = 0; i < 4; ++i) {
#pragma unroll
        for (int j = 0; j < 4; ++j) {
            float lo = bflo(vv[i][j]), hi = bfhi(vv[i][j]);
            o0[2 * j] = fmaf(p0[i], lo, o0[2 * j]); o0[2 * j + 1] = fmaf(p0[i], hi, o0[2 * j + 1]);
            o1[2 * j] = fmaf(p1[i], lo, o1[2 * j]); o1[2 * j + 1] = fmaf(p1[i], hi, o1[2 * j + 1]);
        }
    }
#pragma unroll
    for (int j = 0; j < 8; ++j) {
        o0[j] += __shfl_xor(o0[j], 16); o0[j] += __shfl_xor(o0[j], 32);
        o1[j] += __shfl_xor(o1[j], 16); o1[j] += __shfl_xor(o1[j], 32);
    }
    if (lane < 16) {
#pragma unroll
        for (int j = 0; j < 8; ++j) { opart[wave][0][8 * lane + j] = o0[j]; opart[wave][1][8 * lane + j] = o1[j]; }
    }
    if (lane == 0) { ml[wave][0][0] = m0; ml[wave][0][1] = l0; ml[wave][1][0] = m1; ml[wave][1][1] = l1; }

    float ad[8];
#pragma unroll
    for (int j = 0; j < 8; ++j) ad[j] = 0.f;
#pragma unroll
    for (int j = 0; j < 3; ++j) {
        int r = drg + 7 * j;
        bool ok = (t < 504) && (r < 18);
        float hv = ok ? hs[r] : 0.f;
#pragma unroll
        for (int e = 0; e < 4; ++e) {
            ad[2 * e] = fmaf(hv, bflo(dv[j][e]), ad[2 * e]);
            ad[2 * e + 1] = fmaf(hv, bfhi(dv[j][e]), ad[2 * e + 1]);
        }
    }
    __syncthreads();
    if (t < 504) {
#pragma unroll
        for (int j = 0; j < 8; ++j) Pf[drg][8 * dcc + j] = ad[j];
    }
    __syncthreads();
    if (t < 256) {
        int h = t >> 7, d = t & 127;
        float M = ml[0][h][0];
#pragma unroll
        for (int v = 1; v < 8; ++v) M = fmaxf(M, ml[v][h][0]);
        float o = 0.f, L = 0.f;
#pragma unroll
        for (int v = 0; v < 8; ++v) { float e = __expf(ml[v][h][0] - M); o = fmaf(e, opart[v][h][d], o); L = fmaf(e, ml[v][h][1], L); }
        float* ap = ws.apart + ((size_t)w * 2 + h) * 132;
        ap[d] = o;
        if (d == 0) { ap[128] = M; ap[129] = L; }
    }
    for (int n = t; n < DM; n += NT) {
        float sf = 0.f;
#pragma unroll
        for (int v = 0; v < 7; ++v) sf += Pf[v][n];
        ws.fpart[(size_t)w * DM + n] = sf;
    }
}

__global__ void __launch_bounds__(NT) k3(const __hip_bfloat16* __restrict__ wo, Ws ws) {
    __shared__ float of[16];
    __shared__ float Ps[7][DM];
    const int t = threadIdx.x, w = blockIdx.x;
    const int g = w >> 4, m = w & 15, hh = m >> 3, d0 = 16 * (m & 7);
    const int occ = t % 72, org = t / 72;
    u32x4 ov[3];
#pragma unroll
    for (int j = 0; j < 3; ++j) {
        int r = org + 7 * j;
        bool ok = (t < 504) && (r < 16);
        ov[j] = ok ? ldnt16(wo + (size_t)(16 * w + r) * DM + 8 * occ) : u32x4{0u, 0u, 0u, 0u};
    }
    if (t < 16) {
        float M = -INFINITY;
        for (int s = 0; s < 16; ++s) M = fmaxf(M, ws.apart[((size_t)(16 * g + s) * 2 + hh) * 132 + 128]);
        float o = 0.f, L = 0.f;
        for (int s = 0; s < 16; ++s) {
            const float* ap = ws.apart + ((size_t)(16 * g + s) * 2 + hh) * 132;
            float e = __expf(ap[128] - M);
            o = fmaf(e, ap[d0 + t], o); L = fmaf(e, ap[129], L);
        }
        of[t] = rbf(o / L);
    }
    __syncthreads();
    float as[8];
#pragma unroll
    for (int j = 0; j < 8; ++j) as[j] = 0.f;
#pragma unroll
    for (int j = 0; j < 3; ++j) {
        int r = org + 7 * j;
        bool ok = (t < 504) && (r < 16);
        float hv = ok ? of[r] : 0.f;
#pragma unroll
        for (int e = 0; e < 4; ++e) {
            as[2 * e] = fmaf(hv, bflo(ov[j][e]), as[2 * e]);
            as[2 * e + 1] = fmaf(hv, bfhi(ov[j][e]), as[2 * e + 1]);
        }
    }
    if (t < 504) {
#pragma unroll
        for (int j = 0; j < 8; ++j) Ps[org][8 * occ + j] = as[j];
    }
    __syncthreads();
    for (int n = t; n < DM; n += NT) {
        float ss = 0.f;
#pragma unroll
        for (int v = 0; v < 7; ++v) ss += Ps[v][n];
        ws.spart[(size_t)w * DM + n] = ss;
    }
}

__global__ void __launch_bounds__(256) k4(const __hip_bfloat16* __restrict__ x, Ws ws, __hip_bfloat16* __restrict__ out) {
    __shared__ float red[2][4][4];
    const int t = threadIdx.x, b = blockIdx.x, lane = t & 63, wave = t >> 6;
    float s[4], f[4];
#pragma unroll
    for (int c = 0; c < 4; ++c) { s[c] = ws.spart[(size_t)t * DM + 4 * b + c]; f[c] = ws.fpart[(size_t)t * DM + 4 * b + c]; }
#pragma unroll
    for (int c = 0; c < 4; ++c) { s[c] = wave_sum(s[c]); f[c] = wave_sum(f[c]); }
    if (lane == 0) {
#pragma unroll
        for (int c = 0; c < 4; ++c) { red[0][wave][c] = s[c]; red[1][wave][c] = f[c]; }
    }
    __syncthreads();
    if (t < 4) {
        float ss = (red[0][0][t] + red[0][1][t]) + (red[0][2][t] + red[0][3][t]);
        float ff = (red[1][0][t] + red[1][1][t]) + (red[1][2][t] + red[1][3][t]);
        float sv = rbf(ss), gv = rbf(silu_f(rbf(ff)));
        float xv = __bfloat162float(x[4 * b + t]);
        out[4 * b + t] = __float2bfloat16(xv + rbf(sv * gv));
    }
}

extern "C" void kernel_launch(void* const* d_in, const int* in_sizes, int n_in,
                              void* d_out, int out_size, void* d_ws, size_t ws_size,
                              hipStream_t stream) {
    const __hip_bfloat16* x = (const __hip_bfloat16*)d_in[0];
    const __hip_bfloat16* kc = (const __hip_bfloat16*)d_in[1];
    const __hip_bfloat16* vc = (const __hip_bfloat16*)d_in[2];
    const __hip_bfloat16* wq = (const __hip_bfloat16*)d_in[3];
    const __hip_bfloat16* wo = (const __hip_bfloat16*)d_in[4];
    const __hip_bfloat16* wg = (const __hip_bfloat16*)d_in[6];
    const __hip_bfloat16* wu = (const __hip_bfloat16*)d_in[7];
    const __hip_bfloat16* wd = (const __hip_bfloat16*)d_in[8];
    Ws ws;
    float* base = (float*)d_ws;
    ws.qpart = base;
    ws.gupart = base + 65536;
    ws.apart = ws.gupart + 256 * 576 * 2;
    ws.fpart = ws.apart + 256 * 2 * 132;
    ws.spart = ws.fpart + 256 * 576;
    k1<<<NWG, NT, 0, stream>>>(x, wq, wg, wu, ws);
    k2<<<NWG, NT, 0, stream>>>(kc, vc, wd, ws);
    k3<<<NWG, NT, 0, stream>>>(wo, ws);
    k4<<<144, 256, 0, stream>>>(x, ws, (__hip_bfloat16*)d_out);
}
```

```cpp
#include <hip/hip_runtime.h>
#include <hip/hip_bf16.h>
#include <stdint.h>

typedef unsigned int u32;
typedef unsigned long long u64;
typedef u32 u32x4 __attribute__((ext_vector_type(4)));
typedef int i32x4 __attribute__((ext_vector_type(4)));

constexpr int DM = 576, SK = 8192, DH = 128, WIN = 2048, FF = 4608;
constexpr int NWG = 256;
constexpr int NSW = 8;
constexpr int NTHR = 640;
constexpr int NRED = 144;

constexpr size_t WS_E = 0;
constexpr size_t WS_QG = 4096;
constexpr size_t WS_AG = WS_QG + 256 * 32 * 16;
constexpr size_t WS_GG = WS_AG + 256 * 2 * 72 * 16;
constexpr size_t WS_FG = WS_GG + 256 * 576 * 16;
constexpr size_t WS_END = WS_FG + 256 * 576 * 16;

#define STAMP(k)

constexpr u64 TIMEOUT_TICKS = 2000000ull;

__device__ __forceinline__ float bflo(u32 u) { return __uint_as_float(u << 16); }
__device__ __forceinline__ float bfhi(u32 u) { return __uint_as_float(u & 0xffff0000u); }
__device__ __forceinline__ float rbf(float f) { return __bfloat162float(__float2bfloat16(f)); }
__device__ __forceinline__ float silu_f(float g) { return g / (1.0f + __expf(-g)); }
__device__ __forceinline__ u32x4 ldnt16(const void* p) { return __builtin_nontemporal_load(reinterpret_cast<const u32x4*>(p)); }

template <int CTRL> __device__ __forceinline__ float dppm(float x) {
    return __builtin_bit_cast(float, __builtin_amdgcn_mov_dpp(__builtin_bit_cast(int, x), CTRL, 0xf, 0xf, true));
}
constexpr int DPP_XOR1 = 0xB1, DPP_XOR2 = 0x4E, DPP_HMIR = 0x141, DPP_MIR = 0x140, DPP_ROR4 = 0x124, DPP_ROR8 = 0x128;
__device__ __forceinline__ float row16_sum(float v) {
    v += dppm<DPP_XOR1>(v); v += dppm<DPP_XOR2>(v); v += dppm<DPP_HMIR>(v); v += dppm<DPP_MIR>(v); return v;
}
__device__ __forceinline__ float row16_max(float v) {
    v = fmaxf(v, dppm<DPP_XOR1>(v)); v = fmaxf(v, dppm<DPP_XOR2>(v)); v = fmaxf(v, dppm<DPP_HMIR>(v)); v = fmaxf(v, dppm<DPP_MIR>(v)); return v;
}
__device__ __forceinline__ float x16_sum(float x) {
    auto s = __builtin_amdgcn_permlane16_swap(__float_as_uint(x), __float_as_uint(x), false, false);
    return __uint_as_float(s[0]) + __uint_as_float(s[1]);
}
__device__ __forceinline__ float x32_sum(float x) {
    auto s = __builtin_amdgcn_permlane32_swap(__float_as_uint(x), __float_as_uint(x), false, false);
    return __uint_as_float(s[0]) + __uint_as_float(s[1]);
}
__device__ __forceinline__ float x16_max(float x) {
    auto s = __builtin_amdgcn_permlane16_swap(__float_as_uint(x), __float_as_uint(x), false, false);
    return fmaxf(__uint_as_float(s[0]), __uint_as_float(s[1]));
}
__device__ __forceinline__ float x32_max(float x) {
    auto s = __builtin_amdgcn_permlane32_swap(__float_as_uint(x), __float_as_uint(x), false, false);
    return fmaxf(__uint_as_float(s[0]), __uint_as_float(s[1]));
}
__device__ __forceinline__ float wave_sum(float v) { return x32_sum(x16_sum(row16_sum(v))); }

struct Smem {
    float xn[DM];
    float xraw[DM];
    float qp[NSW][64];
    float qs[2 * DH];
    float opart[NSW][2][DH];
    float ml[NSW][2][2];
    float Pg[7][DM];
    float Pu[7][DM];
    float Ps[7][DM];
    float Pf[7][DM];
    float tmpg[DM];
    float tmpu[DM];
    float hs[32];
    float of[16];
    float red[NSW][4][2];
    u32 sync[16];
    u32 tag[2];
};
enum { CNT_QP = 0, FLAG_Q = 1, CNT_ATT = 2, FLAG_O = 3, CNT_GU = 4, FLAG_H = 5, CNT_FIN = 6, CNT_RED = 7, CNT_GUPUB = 8, ABORTW = 15 };

__device__ __forceinline__ u32 lds_ld(u32* p) { return __hip_atomic_load(p, __ATOMIC_RELAXED, __HIP_MEMORY_SCOPE_WORKGROUP); }

__device__ __forceinline__ void wait_lds(Smem& sm, int idx, u32 target) {
    u32 it = 0;
    u64 t0 = 0;
    while (lds_ld(&sm.sync[idx]) < target) {
        __builtin_amdgcn_s_sleep(1);
        if ((++it & 255u) == 0u) {
            if (lds_ld(&sm.sync[ABORTW]) != 0u) break;
            u64 now = __builtin_amdgcn_s_memrealtime();
            if (t0 == 0) t0 = now;
            else if (now - t0 > TIMEOUT_TICKS) { __hip_atomic_store(&sm.sync[ABORTW], 1u, __ATOMIC_RELAXED, __HIP_MEMORY_SCOPE_WORKGROUP); break; }
        }
    }
    __builtin_amdgcn_fence(__ATOMIC_ACQUIRE, "workgroup");
}
__device__ __forceinline__ void arrive_lds(Smem& sm, int idx, int lane) {
    __builtin_amdgcn_fence(__ATOMIC_RELEASE, "workgroup");
    if (lane == 0) __hip_atomic_fetch_add(&sm.sync[idx], 1u, __ATOMIC_RELAXED, __HIP_MEMORY_SCOPE_WORKGROUP);
}
__device__ __forceinline__ void set_lds(Smem& sm, int idx, int lane) {
    __builtin_amdgcn_fence(__ATOMIC_RELEASE, "workgroup");
    if (lane == 0) __hip_atomic_store(&sm.sync[idx], 1u, __ATOMIC_RELAXED, __HIP_MEMORY_SCOPE_WORKGROUP);
}

typedef __amdgpu_buffer_rsrc_t rsrc_t;
__device__ __forceinline__ u32x4 gran_load(rsrc_t r, u32 off) {
    return __builtin_bit_cast(u32x4, __builtin_amdgcn_raw_buffer_load_b128(r, (int)off, 0, 16));
}
__device__ __forceinline__ u32x4 bld(rsrc_t r, u32 off) {
    return __builtin_bit_cast(u32x4, __builtin_amdgcn_raw_buffer_load_b128(r, (int)off, 0, 2));
}
constexpr u32 OOB = 0xFFFFFF00u;
__device__ __forceinline__ void gran_store(rsrc_t r, u32 off, u32x4 v) {
    __builtin_amdgcn_raw_buffer_store_b128(__builtin_bit_cast(i32x4, v), r, (int)off, 0, 16);
}

__device__ __forceinline__ bool spin_check(Smem& sm, u32& it, u64& t0) {
    __builtin_amdgcn_s_sleep(2);
    if ((++it & 63u) == 0u) {
        if (lds_ld(&sm.sync[ABORTW]) != 0u) return true;
        u64 now = __builtin_amdgcn_s_memrealtime();
        if (t0 == 0) t0 = now;
        else if (now - t0 > TIMEOUT_TICKS) { __hip_atomic_store(&sm.sync[ABORTW], 1u, __ATOMIC_RELAXED, __HIP_MEMORY_SCOPE_WORKGROUP); return true; }
    }
    return false;
}

__global__ void __launch_bounds__(NTHR, 3) fused_kernel(
    const __hip_bfloat16* __restrict__ x, const __hip_bfloat16* __restrict__ kc, const __hip_bfloat16* __restrict__ vc,
    const __hip_bfloat16* __restrict__ wq, const __hip_bfloat16* __restrict__ wo, const __hip_bfloat16* __restrict__ wg,
    const __hip_bfloat16* __restrict__ wu, const __hip_bfloat16* __restrict__ wd,
    __hip_bfloat16* __restrict__ out, unsigned char* ws) {
    __shared__ Smem sm;
    const int t = threadIdx.x, lane = t & 63, wave = t >> 6, w = blockIdx.x;
    const rsrc_t rs = __builtin_amdgcn_make_buffer_rsrc(ws, 0, (int)WS_END, 0x00020000);
    const rsrc_t r_wq = __builtin_amdgcn_make_buffer_rsrc((void*)wq, 0, DM * 4096 * 2, 0x00020000);
    const rsrc_t r_k = __builtin_amdgcn_make_buffer_rsrc((void*)kc, 0, 16 * SK * DH * 2, 0x00020000);
    const rsrc_t r_v = __builtin_amdgcn_make_buffer_rsrc((void*)vc, 0, 16 * SK * DH * 2, 0x00020000);
    const rsrc_t r_wg = __builtin_amdgcn_make_buffer_rsrc((void*)wg, 0, DM * FF * 2, 0x00020000);
    const rsrc_t r_wu = __builtin_amdgcn_make_buffer_rsrc((void*)wu, 0, DM * FF * 2, 0x00020000);
    const rsrc_t r_wd = __builtin_amdgcn_make_buffer_rsrc((void*)wd, 0, DM * FF * 2, 0x00020000);
    const rsrc_t r_wo = __builtin_amdgcn_make_buffer_rsrc((void*)wo, 0, DM * 4096 * 2, 0x00020000);

    if (wave == 0) STAMP(0);
    if (t < 16) sm.sync[t] = 0u;

    if (wave < NSW) {
        const rsrc_t r_x = __builtin_amdgcn_make_buffer_rsrc((void*)x, 0, DM * 2, 0x00020000);
        u32x4 xa = __builtin_bit_cast(u32x4, __builtin_amdgcn_raw_buffer_load_b128(r_x, lane * 16, 0, 0));
        u32x4 xb = __builtin_bit_cast(u32x4, __builtin_amdgcn_raw_buffer_load_b128(r_x, 1024 + lane * 16, 0, 0));

        const int qcb = w >> 2, qks = w & 3;
        const int cc = t & 7, r0 = t >> 3;
        u32x4 wqv[3];
#pragma unroll
        for (int i = 0; i < 3; ++i) {
            int r = r0 + 64 * i;
            wqv[i] = bld(r_wq, r < 144 ? (u32)(((144 * qks + r) * 4096 + 64 * qcb + 8 * cc) * 2) : OOB);
        }
        const int g = w >> 4, s = w & 15;
        const size_t kvbase = ((size_t)g * SK + (SK - WIN) + 128 * s) * DH;
        u32x4 kv[4], vv[4];
#pragma unroll
        for (int i = 0; i < 4; ++i) {
            int key = 16 * wave + 4 * i + (lane >> 4);
            kv[i] = bld(r_k, (u32)((kvbase + (size_t)key * DH + 8 * (lane & 15)) * 2));
        }
        const int gcb = w >> 5, gks = w & 31;
        const int pcc = t % 72, prg = t / 72;
        const bool pact = t < 504;
        u32x4 gv[3], uv[3], dv[3], ov[3];
        asm volatile("" ::: "memory");
        if (wave == 0) STAMP(16);
        {
            float va[8], vb[8];
#pragma unroll
            for (int j = 0; j < 4; ++j) { va[2 * j] = bflo(xa[j]); va[2 * j + 1] = bfhi(xa[j]); vb[2 * j] = bflo(xb[j]); vb[2 * j + 1] = bfhi(xb[j]); }
            float sum = 0.f;
#pragma unroll
            for (int j = 0; j < 8; ++j) sum += va[j];
            if (lane < 8) {
#pragma unroll
                for (int j = 0; j < 8; ++j) sum += vb[j];
            }
            sum = wave_sum(sum);
            if (wave == 0) STAMP(17);
            const float mean = sum * (1.0f / DM);
            float qv = 0.f;
#pragma unroll
            for (int j = 0; j < 8; ++j) { float d = va[j] - mean; qv += d * d; }
            if (lane < 8) {
#pragma unroll
                for (int j = 0; j < 8; ++j) { float d = vb[j] - mean; qv += d * d; }
            }
            qv = wave_sum(qv);
            const float var = rbf(qv * (1.0f / DM));
            const float m = rbf(mean);
            const float den = rbf(sqrtf(rbf(var + 1e-5f)));
#pragma unroll
            for (int j = 0; j < 8; ++j) { sm.xn[8 * lane + j] = rbf(rbf(va[j] - m) / den); sm.xraw[8 * lane + j] = va[j]; }
            if (lane < 8) {
#pragma unroll
                for (int j = 0; j < 8; ++j) { sm.xn[512 + 8 * lane + j] = rbf(rbf(vb[j] - m) / den); sm.xraw[512 + 8 * lane + j] = vb[j]; }
            }
        }
        if (wave == 0) STAMP(18);
        __syncthreads();
        if (wave == 0) STAMP(1);

        {
            float acc[8];
#pragma unroll
            for (int j = 0; j < 8; ++j) acc[j] = 0.f;
#pragma unroll
            for (int i = 0; i < 3; ++i) {
                int r = r0 + 64 * i;
                float xv = (r < 144) ? sm.xn[144 * qks + r] : 0.f;
#pragma unroll
                for (int j = 0; j < 4; ++j) {
                    acc[2 * j] = fmaf(xv, bflo(wqv[i][j]), acc[2 * j]);
                    acc[2 * j + 1] = fmaf(xv, bfhi(wqv[i][j]), acc[2 * j + 1]);
                }
            }
#pragma unroll
            for (int j = 0; j < 8; ++j) {
                acc[j] += dppm<DPP_ROR8>(acc[j]);
                acc[j] = x32_sum(x16_sum(acc[j]));
            }
            if (lane < 8) {
#pragma unroll
                for (int j = 0; j < 8; ++j) sm.qp[wave][8 * lane + j] = acc[j];
            }
            arrive_lds(sm, CNT_QP, lane);
        }
        if (wave == 0) STAMP(2);
        asm volatile("" ::: "memory");
#pragma unroll
        for (int i = 0; i < 4; ++i) {
            int key = 16 * wave + 4 * i + (lane >> 4);
            vv[i] = bld(r_v, (u32)((kvbase + (size_t)key * DH + 8 * (lane & 15)) * 2));
        }
        asm volatile("s_waitcnt vmcnt(4)" ::: "memory");
#pragma unroll
        for (int j = 0; j < 3; ++j) {
            int r = prg + 7 * j;
            size_t off = (size_t)(18 * gks + r) * FF + 576 * gcb + 8 * pcc;
            gv[j] = bld(r_wg, (pact && r < 18) ? (u32)(off * 2) : OOB);
        }
        asm volatile("s_waitcnt vmcnt(3)" ::: "memory");
#pragma unroll
        for (int j = 0; j < 3; ++j) {
            int r = prg + 7 * j;
            size_t off = (size_t)(18 * gks + r) * FF + 576 * gcb + 8 * pcc;
            uv[j] = bld(r_wu, (pact && r < 18) ? (u32)(off * 2) : OOB);
        }
        asm volatile("" ::: "memory");

        wait_lds(sm, FLAG_Q, 1u);
        const u64 TT = (u64)sm.tag[0] | ((u64)sm.tag[1] << 32);
        if (wave == 0) STAMP(3);
        {
            float q0[8], q1[8];
#pragma unroll
            for (int j = 0; j < 8; ++j) { q0[j] = sm.qs[8 * (lane & 15) + j]; q1[j] = sm.qs[DH + 8 * (lane & 15) + j]; }
            float sc0[4], sc1[4];
#pragma unroll
            for (int i = 0; i < 4; ++i) {
                float a0 = 0.f, a1 = 0.f;
#pragma unroll
                for (int j = 0; j < 4; ++j) {
                    float lo = bflo(kv[i][j]), hi = bfhi(kv[i][j]);
                    a0 = fmaf(q0[2 * j], lo, a0); a0 = fmaf(q0[2 * j + 1], hi, a0);
                    a1 = fmaf(q1[2 * j], lo, a1); a1 = fmaf(q1[2 * j + 1], hi, a1);
                }
                a0 = row16_sum(a0); a1 = row16_sum(a1);
                sc0[i] = rbf(rbf(a0) * 0.08838834764831845f);
                sc1[i] = rbf(rbf(a1) * 0.08838834764831845f);
            }
            float m0 = fmaxf(fmaxf(sc0[0], sc0[1]), fmaxf(sc0[2], sc0[3]));
            float m1 = fmaxf(fmaxf(sc1[0], sc1[1]), fmaxf(sc1[2], sc1[3]));
            m0 = x32_max(x16_max(m0));
            m1 = x32_max(x16_max(m1));
            float p0[4], p1[4], l0 = 0.f, l1 = 0.f;
#pragma unroll
            for (int i = 0; i < 4; ++i) { p0[i] = __expf(sc0[i] - m0); p1[i] = __expf(sc1[i] - m1); l0 += p0[i]; l1 += p1[i]; }
            l0 = x32_sum(x16_sum(l0));
            l1 = x32_sum(x16_sum(l1));
            float o0[8], o1[8];
#pragma unroll
            for (int j = 0; j < 8; ++j) { o0[j] = 0.f; o1[j] = 0.f; }
#pragma unroll
            for (int i = 0; i < 4; ++i) {
#pragma unroll
                for (int j = 0; j < 4; ++j) {
                    float lo = bflo(vv[i][j]), hi = bfhi(vv[i][j]);
                    o0[2 * j] = fmaf(p0[i], lo, o0[2 * j]); o0[2 * j + 1] = fmaf(p0[i], hi, o0[2 * j + 1]);
                    o1[2 * j] = fmaf(p1[i], lo, o1[2 * j]); o1[2 * j + 1] = fmaf(p1[i], hi, o1[2 * j + 1]);
                }
            }
#pragma unroll
            for (int j = 0; j < 8; ++j) {
                o0[j] = x32_sum(x16_sum(o0[j]));
                o1[j] = x32_sum(x16_sum(o1[j]));
            }
            if (lane < 16) {
#pragma unroll
                for (int j = 0; j < 8; ++j) { sm.opart[wave][0][8 * lane + j] = o0[j]; sm.opart[wave][1][8 * lane + j] = o1[j]; }
            }
            if (lane == 0) { sm.ml[wave][0][0] = m0; sm.ml[wave][0][1] = l0; sm.ml[wave][1][0] = m1; sm.ml[wave][1][1] = l1; }
            arrive_lds(sm, CNT_ATT, lane);
        }
        if (wave == 0) STAMP(4);
        asm volatile("" ::: "memory");
#pragma unroll
        for (int j = 0; j < 3; ++j) {
            int r = prg + 7 * j;
            dv[j] = bld(r_wd, (pact && r < 18) ? (u32)(((18 * w + r) * DM + 8 * pcc) * 2) : OOB);
        }
#pragma unroll
        for (int j = 0; j < 3; ++j) {
            int r = prg + 7 * j;
            ov[j] = bld(r_wo, (pact && r < 16) ? (u32)(((16 * w + r) * DM + 8 * pcc) * 2) : OOB);
        }
        asm volatile("" ::: "memory");

        {
            float ag[8], au[8];
#pragma unroll
            for (int j = 0; j < 8; ++j) { ag[j] = 0.f; au[j] = 0.f; }
#pragma unroll
            for (int j = 0; j < 3; ++j) {
                int r = prg + 7 * j;
                bool ok = pact && (r < 18);
                float xv = ok ? sm.xn[18 * gks + r] : 0.f;
#pragma unroll
                for (int e = 0; e < 4; ++e) {
                    ag[2 * e] = fmaf(xv, bflo(gv[j][e]), ag[2 * e]);
                    ag[2 * e + 1] = fmaf(xv, bfhi(gv[j][e]), ag[2 * e + 1]);
                    au[2 * e] = fmaf(xv, bflo(uv[j][e]), au[2 * e]);
                    au[2 * e + 1] = fmaf(xv, bfhi(uv[j][e]), au[2 * e + 1]);
                }
            }
            if (pact) {
#pragma unroll
                for (int j = 0; j < 8; ++j) { sm.Pg[prg][8 * pcc + j] = ag[j]; sm.Pu[prg][8 * pcc + j] = au[j]; }
            }
            arrive_lds(sm, CNT_GU, lane);
            wait_lds(sm, CNT_GU, NSW);
            for (int n = t; n < DM; n += NSW * 64) {
                float sg = 0.f, su = 0.f;
#pragma unroll
                for (int v = 0; v < 7; ++v) { sg += sm.Pg[v][n]; su += sm.Pu[v][n]; }
                u32x4 gr = {__float_as_uint(sg), (u32)TT, __float_as_uint(su), (u32)(TT >> 32)};
                gran_store(rs, (u32)(WS_GG + ((size_t)w * DM + n) * 16), gr);
            }
            arrive_lds(sm, CNT_GUPUB, lane);
        }
        if (wave == 0) STAMP(5);

        wait_lds(sm, FLAG_H, 1u);
        if (wave == 0) STAMP(6);
        {
            float ad[8];
#pragma unroll
            for (int j = 0; j < 8; ++j) ad[j] = 0.f;
#pragma unroll
            for (int j = 0; j < 3; ++j) {
                int r = prg + 7 * j;
                bool ok = pact && (r < 18);
                float hv = ok ? sm.hs[r] : 0.f;
#pragma unroll
                for (int e = 0; e < 4; ++e) {
                    ad[2 * e] = fmaf(hv, bflo(dv[j][e]), ad[2 * e]);
                    ad[2 * e + 1] = fmaf(hv, bfhi(dv[j][e]), ad[2 * e + 1]);
                }
            }
            if (pact) {
#pragma unroll
                for (int j = 0; j < 8; ++j) sm.Pf[prg][8 * pcc + j] = ad[j];
            }
        }
        wait_lds(sm, FLAG_O, 1u);
        if (wave == 0) STAMP(7);
        {
            float as[8];
#pragma unroll
            for (int j = 0; j < 8; ++j) as[j] = 0.f;
#pragma unroll
            for (int j = 0; j < 3; ++j) {
                int r = prg + 7 * j;
                bool ok = pact && (r < 16);
                float hv = ok ? sm.of[r] : 0.f;
#pragma unroll
                for (int e = 0; e < 4; ++e) {
                    as[2 * e] = fmaf(hv, bflo(ov[j][e]), as[2 * e]);
                    as[2 * e + 1] = fmaf(hv, bfhi(ov[j][e]), as[2 * e + 1]);
                }
            }
            if (pact) {
#pragma unroll
                for (int j = 0; j < 8; ++j) sm.Ps[prg][8 * pcc + j] = as[j];
            }
            arrive_lds(sm, CNT_FIN, lane);
            wait_lds(sm, CNT_FIN, NSW);
            for (int n = t; n < DM; n += NSW * 64) {
                float ss = 0.f, sf = 0.f;
#pragma unroll
                for (int v = 0; v < 7; ++v) { ss += sm.Ps[v][n]; sf += sm.Pf[v][n]; }
                u32x4 gr = {__float_as_uint(ss), (u32)TT, __float_as_uint(sf), (u32)(TT >> 32)};
                gran_store(rs, (u32)(WS_FG + ((size_t)w * DM + n) * 16), gr);
            }
        }
        if (wave == 0) STAMP(8);

        if (w < NRED) {
            const int c = lane & 3;
            const int p0 = 32 * wave + (lane >> 2);
            const u32 off0 = (u32)(WS_FG + ((size_t)p0 * DM + 4 * w + c) * 16);
            const u32 off1 = (u32)(WS_FG + ((size_t)(p0 + 16) * DM + 4 * w + c) * 16);
            u32x4 a, b;
            u32 it = 0; u64 t0 = 0;
            while (true) {
                asm volatile("" ::: "memory");
                a = gran_load(rs, off0);
                b = gran_load(rs, off1);
                bool ok = (a.y == (u32)TT) && (a.w == (u32)(TT >> 32)) && (b.y == (u32)TT) && (b.w == (u32)(TT >> 32));
                if (__all(ok)) break;
                if (spin_check(sm, it, t0)) break;
            }
            float sv = __uint_as_float(a.x) + __uint_as_float(b.x);
            float fv = __uint_as_float(a.z) + __uint_as_float(b.z);
            sv += dppm<DPP_ROR4>(sv); fv += dppm<DPP_ROR4>(fv);
            sv += dppm<DPP_ROR8>(sv); fv += dppm<DPP_ROR8>(fv);
            sv = x32_sum(x16_sum(sv)); fv = x32_sum(x16_sum(fv));
            if (lane < 4) { sm.red[wave][lane][0] = sv; sm.red[wave][lane][1] = fv; }
            arrive_lds(sm, CNT_RED, lane);
            if (wave == 0) {
                wait_lds(sm, CNT_RED, NSW);
                if (lane < 4) {
                    float ss = 0.f, ff = 0.f;
#pragma unroll
                    for (int v = 0; v < NSW; ++v) { ss += sm.red[v][lane][0]; ff += sm.red[v][lane][1]; }
                    float sb = rbf(ss), gt = rbf(silu_f(rbf(ff)));
                    out[4 * w + lane] = __float2bfloat16(sm.xraw[4 * w + lane] + rbf(sb * gt));
                }
                if (w == 0 && lane == 0) __hip_atomic_store((u64*)(ws + WS_E), TT, __ATOMIC_RELAXED, __HIP_MEMORY_SCOPE_AGENT);
                STAMP(9);
            }
        }
    } else {
        const u64 E0 = __hip_atomic_load((u64*)(ws + WS_E), __ATOMIC_RELAXED, __HIP_MEMORY_SCOPE_AGENT);
        const u64 TT = E0 + 1ull;
        __syncthreads();
        const u32 tagA = (u32)TT, tagB = (u32)(TT >> 32);
        if (wave == NSW) {
            const int g = w >> 4, m = w & 15;
            if (lane == 0) { sm.tag[0] = tagA; sm.tag[1] = tagB; }
            wait_lds(sm, CNT_QP, NSW);
            STAMP(10);
            if (lane < 32) {
                float v0 = 0.f, v1 = 0.f;
#pragma unroll
                for (int v = 0; v < NSW; ++v) { v0 += sm.qp[v][2 * lane]; v1 += sm.qp[v][2 * lane + 1]; }
                u32x4 gr = {__float_as_uint(v0), tagA, __float_as_uint(v1), tagB};
                gran_store(rs, (u32)(WS_QG + ((size_t)w * 32 + lane) * 16), gr);
            }
            {
                const int p = lane & 31, hi = lane >> 5;
                u32x4 gq[8];
                u32 it = 0; u64 t0 = 0;
                while (true) {
                    asm volatile("" ::: "memory");
                    bool ok = true;
#pragma unroll
                    for (int k = 0; k < 8; ++k) {
                        gq[k] = gran_load(rs, (u32)(WS_QG + ((size_t)(16 * g + 2 * k + hi) * 32 + p) * 16));
                    }
#pragma unroll
                    for (int k = 0; k < 8; ++k) ok &= (gq[k].y == tagA) && (gq[k].w == tagB);
                    if (__all(ok)) break;
                    if (spin_check(sm, it, t0)) break;
                }
#pragma unroll
                for (int cb = 0; cb < 4; ++cb) {
                    float a0 = __uint_as_float(gq[2 * cb].x) + __uint_as_float(gq[2 * cb + 1].x);
                    float a1 = __uint_as_float(gq[2 * cb].z) + __uint_as_float(gq[2 * cb + 1].z);
                    a0 = x32_sum(a0);
                    a1 = x32_sum(a1);
                    if (hi == 0) { sm.qs[64 * cb + 2 * p] = rbf(a0); sm.qs[64 * cb + 2 * p + 1] = rbf(a1); }
                }
            }
            set_lds(sm, FLAG_Q, lane);
            STAMP(11);

            wait_lds(sm, CNT_ATT, NSW);
            {
#pragma unroll
                for (int h = 0; h < 2; ++h) {
                    float M = sm.ml[0][h][0];
#pragma unroll
                    for (int v = 1; v < NSW; ++v) M = fmaxf(M, sm.ml[v][h][0]);
                    float oa = 0.f, ob = 0.f, L = 0.f;
#pragma unroll
                    for (int v = 0; v < NSW; ++v) {
                        float e = __expf(sm.ml[v][h][0] - M);
                        oa = fmaf(e, sm.opart[v][h][2 * lane], oa);
                        ob = fmaf(e, sm.opart[v][h][2 * lane + 1], ob);
                        L = fmaf(e, sm.ml[v][h][1], L);
                    }
                    u32x4 gr = {__float_as_uint(oa), tagA, __float_as_uint(ob), tagB};
                    gran_store(rs, (u32)(WS_AG + (((size_t)w * 2 + h) * 72 + lane) * 16), gr);
                    if (lane == 0) {
                        u32x4 g2 = {__float_as_uint(M), tagA, __float_as_uint(L), tagB};
                        gran_store(rs, (u32)(WS_AG + (((size_t)w * 2 + h) * 72 + 64) * 16), g2);
                    }
                }
            }
            STAMP(12);
            {
                const int hh = m >> 3, pp0 = 8 * (m & 7), sp = lane & 15, part = lane >> 4;
                const size_t base = ((size_t)(16 * g + sp) * 2 + hh) * 72;
                u32x4 ga, gb, gm;
                u32 it = 0; u64 t0 = 0;
                while (true) {
                    asm volatile("" ::: "memory");
                    ga = gran_load(rs, (u32)(WS_AG + (base + pp0 + 2 * part) * 16));
                    gb = gran_load(rs, (u32)(WS_AG + (base + pp0 + 2 * part + 1) * 16));
                    gm = gran_load(rs, (u32)(WS_AG + (base + 64) * 16));
                    bool ok = (ga.y == tagA) && (ga.w == tagB) && (gb.y == tagA) && (gb.w == tagB) && (gm.y == tagA) && (gm.w == tagB);
                    if (__all(ok)) break;
                    if (spin_check(sm, it, t0)) break;
                }
                float ms = __uint_as_float(gm.x), ls = __uint_as_float(gm.z);
                float M = ms;
                M = row16_max(M);
                float e = __expf(ms - M);
                float L = e * ls;
                float a0 = e * __uint_as_float(ga.x), a1 = e * __uint_as_float(ga.z), a2 = e * __uint_as_float(gb.x), a3 = e * __uint_as_float(gb.z);
                L = row16_sum(L); a0 = row16_sum(a0); a1 = row16_sum(a1); a2 = row16_sum(a2); a3 = row16_sum(a3);
                if (sp == 0) {
                    sm.of[4 * part + 0] = rbf(a0 / L); sm.of[4 * part + 1] = rbf(a1 / L);
                    sm.of[4 * part + 2] = rbf(a2 / L); sm.of[4 * part + 3] = rbf(a3 / L);
                }
            }
            set_lds(sm, FLAG_O, lane);
            STAMP(13);
        } else {
            const int gcb = w >> 5, c0 = 18 * (w & 31);
            wait_lds(sm, CNT_GUPUB, NSW);
            STAMP(14);
            u32x4 gg[9];
            u32 it = 0; u64 t0 = 0;
            while (true) {
                asm volatile("" ::: "memory");
                bool ok = true;
#pragma unroll
                for (int k = 0; k < 9; ++k) {
                    int i = 64 * k + lane; int ks = i / 18, j = i - 18 * ks;
                    gg[k] = gran_load(rs, (u32)(WS_GG + ((size_t)(32 * gcb + ks) * DM + c0 + j) * 16));
                }
#pragma unroll
                for (int k = 0; k < 9; ++k) ok &= (gg[k].y == tagA) && (gg[k].w == tagB);
                if (__all(ok)) break;
                if (spin_check(sm, it, t0)) break;
            }
#pragma unroll
            for (int k = 0; k < 9; ++k) { int i = 64 * k + lane; sm.tmpg[i] = __uint_as_float(gg[k].x); sm.tmpu[i] = __uint_as_float(gg[k].z); }
            __builtin_amdgcn_fence(__ATOMIC_RELEASE, "workgroup");
            __builtin_amdgcn_wave_barrier();
            if (lane < 18) {
                float sg = 0.f, su = 0.f;
#pragma unroll 8
                for (int ks = 0; ks < 32; ++ks) { sg += sm.tmpg[18 * ks + lane]; su += sm.tmpu[18 * ks + lane]; }
                float gt = rbf(sg), up = rbf(su);
                sm.hs[lane] = rbf(rbf(silu_f(gt)) * up);
            }
            set_lds(sm, FLAG_H, lane);
            STAMP(15);
        }
    }
}

extern "C" void kernel_launch(void* const* d_in, const int* in_sizes, int n_in,
                              void* d_out, int out_size, void* d_ws, size_t ws_size,
                              hipStream_t stream) {
    fused_kernel<<<NWG, NTHR, 0, stream>>>(
        (const __hip_bfloat16*)d_in[0], (const __hip_bfloat16*)d_in[1], (const __hip_bfloat16*)d_in[2],
        (const __hip_bfloat16*)d_in[3], (const __hip_bfloat16*)d_in[4], (const __hip_bfloat16*)d_in[6],
        (const __hip_bfloat16*)d_in[7], (const __hip_bfloat16*)d_in[8], (__hip_bfloat16*)d_out, (unsigned char*)d_ws);
}
```

```cpp
#include <hip/hip_runtime.h>
#include <hip/hip_bf16.h>
#include <stdint.h>

typedef unsigned int u32;
typedef unsigned long long u64;
typedef u32 u32x4 __attribute__((ext_vector_type(4)));
typedef int i32x4 __attribute__((ext_vector_type(4)));
typedef float f32x4 __attribute__((ext_vector_type(4)));
typedef __bf16 bf16x2 __attribute__((ext_vector_type(2)));

constexpr int DM = 576, SK = 8192, DH = 128, WIN = 2048, FF = 4608;
constexpr int NWG = 256;
constexpr int NAW = 8, NFW = 4;
constexpr int NTHR = (NAW + NFW + 2) * 64;
constexpr int NRED = 144;

constexpr size_t WS_E = 0;
constexpr size_t WS_QG = 4096;
constexpr size_t WS_AG = WS_QG + 256 * 32 * 16;
constexpr size_t WS_GG = WS_AG + 256 * 2 * 72 * 16;
constexpr size_t WS_FG = WS_GG + 256 * 576 * 16;
constexpr size_t WS_END = WS_FG + 256 * 576 * 16;

#define STAMP(k)

constexpr u64 TIMEOUT_TICKS = 2000000ull;

__device__ __forceinline__ float bflo(u32 u) { return __uint_as_float(u << 16); }
__device__ __forceinline__ float bfhi(u32 u) { return __uint_as_float(u & 0xffff0000u); }
__device__ __forceinline__ float rbf(float f) { return __bfloat162float(__float2bfloat16(f)); }
__device__ __forceinline__ float silu_f(float g) { return g / (1.0f + __expf(-g)); }

__device__ __forceinline__ float dot2(u32 a, u32 b, float acc) {
    return __builtin_amdgcn_fdot2_f32_bf16(__builtin_bit_cast(bf16x2, a), __builtin_bit_cast(bf16x2, b), acc, false);
}
__device__ __forceinline__ void axpy8(float (&acc)[8], const u32x4& w, float xv) {
    const u32 lo = __float_as_uint(xv) >> 16, hi = lo << 16;
#pragma unroll
    for (int j = 0; j < 4; ++j) { acc[2 * j] = dot2(w[j], lo, acc[2 * j]); acc[2 * j + 1] = dot2(w[j], hi, acc[2 * j + 1]); }
}
__device__ __forceinline__ void st8(float* p, const float (&a)[8]) {
    *reinterpret_cast<f32x4*>(p) = f32x4{a[0], a[1], a[2], a[3]};
    *reinterpret_cast<f32x4*>(p + 4) = f32x4{a[4], a[5], a[6], a[7]};
}
template <int CTRL> __device__ __forceinline__ float dppm(float x) {
    return __builtin_bit_cast(float, __builtin_amdgcn_mov_dpp(__builtin_bit_cast(int, x), CTRL, 0xf, 0xf, true));
}
constexpr int DPP_XOR1 = 0xB1, DPP_XOR2 = 0x4E, DPP_HMIR = 0x141, DPP_MIR = 0x140, DPP_ROR4 = 0x124, DPP_ROR8 = 0x128;
__device__ __forceinline__ float row16_sum(float v) {
    v += dppm<DPP_XOR1>(v); v += dppm<DPP_XOR2>(v); v += dppm<DPP_HMIR>(v); v += dppm<DPP_MIR>(v); return v;
}
__device__ __forceinline__ float row16_max(float v) {
    v = fmaxf(v, dppm<DPP_XOR1>(v)); v = fmaxf(v, dppm<DPP_XOR2>(v)); v = fmaxf(v, dppm<DPP_HMIR>(v)); v = fmaxf(v, dppm<DPP_MIR>(v)); return v;
}
__device__ __forceinline__ float x16_sum(float x) {
    auto s = __builtin_amdgcn_permlane16_swap(__float_as_uint(x), __float_as_uint(x), false, false);
    return __uint_as_float(s[0]) + __uint_as_float(s[1]);
}
__device__ __forceinline__ float x32_sum(float x) {
    auto s = __builtin_amdgcn_permlane32_swap(__float_as_uint(x), __float_as_uint(x), false, false);
    return __uint_as_float(s[0]) + __uint_as_float(s[1]);
}
__device__ __forceinline__ float x16_max(float x) {
    auto s = __builtin_amdgcn_permlane16_swap(__float_as_uint(x), __float_as_uint(x), false, false);
    return fmaxf(__uint_as_float(s[0]), __uint_as_float(s[1]));
}
__device__ __forceinline__ float x32_max(float x) {
    auto s = __builtin_amdgcn_permlane32_swap(__float_as_uint(x), __float_as_uint(x), false, false);
    return fmaxf(__uint_as_float(s[0]), __uint_as_float(s[1]));
}
__device__ __forceinline__ float wave_sum(float v) { return x32_sum(x16_sum(row16_sum(v))); }

struct __attribute__((aligned(16))) Smem {
    u32 qsb[DH];
    float xn[DM];
    float xraw[DM];
    float qp[NAW][64];
    float opart[NAW][2][DH];
    float ml[NAW][2][2];
    float Pg[3][DM];
    float Pu[3][DM];
    float Ps[7][DM];
    float Pf[3][DM];
    float tmpg[DM];
    float tmpu[DM];
    float hs[32];
    float of[16];
    float red[NAW][4][2];
    u32 sync[16];
    u32 tag[2];
};
enum { CNT_QP = 0, FLAG_Q = 1, CNT_ATT = 2, FLAG_O = 3, CNT_GU = 4, FLAG_H = 5, CNT_FIN = 6, CNT_RED = 7, CNT_GUPUB = 8, FLAG_TAG = 9, ABORTW = 15 };

__device__ __forceinline__ u32 lds_ld(u32* p) { return __hip_atomic_load(p, __ATOMIC_RELAXED, __HIP_MEMORY_SCOPE_WORKGROUP); }

__device__ __forceinline__ void wait_lds(Smem& sm, int idx, u32 target) {
    u32 it = 0;
    u64 t0 = 0;
    while (lds_ld(&sm.sync[idx]) < target) {
        __builtin_amdgcn_s_sleep(1);
        if ((++it & 255u) == 0u) {
            if (lds_ld(&sm.sync[ABORTW]) != 0u) break;
            u64 now = __builtin_amdgcn_s_memrealtime();
            if (t0 == 0) t0 = now;
            else if (now - t0 > TIMEOUT_TICKS) { __hip_atomic_store(&sm.sync[ABORTW], 1u, __ATOMIC_RELAXED, __HIP_MEMORY_SCOPE_WORKGROUP); break; }
        }
    }
    __builtin_amdgcn_fence(__ATOMIC_ACQUIRE, "workgroup");
}
__device__ __forceinline__ void arrive_lds(Smem& sm, int idx, int lane) {
    __builtin_amdgcn_fence(__ATOMIC_RELEASE, "workgroup");
    if (lane == 0) __hip_atomic_fetch_add(&sm.sync[idx], 1u, __ATOMIC_RELAXED, __HIP_MEMORY_SCOPE_WORKGROUP);
}
__device__ __forceinline__ void set_lds(Smem& sm, int idx, int lane) {
    __builtin_amdgcn_fence(__ATOMIC_RELEASE, "workgroup");
    if (lane == 0) __hip_atomic_store(&sm.sync[idx], 1u, __ATOMIC_RELAXED, __HIP_MEMORY_SCOPE_WORKGROUP);
}

typedef __amdgpu_buffer_rsrc_t rsrc_t;
__device__ __forceinline__ u32x4 gran_load(rsrc_t r, u32 off) {
    return __builtin_bit_cast(u32x4, __builtin_amdgcn_raw_buffer_load_b128(r, (int)off, 0, 16));
}
__device__ __forceinline__ void gran_store(rsrc_t r, u32 off, u32x4 v) {
    __builtin_amdgcn_raw_buffer_store_b128(__builtin_bit_cast(i32x4, v), r, (int)off, 0, 16);
}
__device__ __forceinline__ u32x4 bld(rsrc_t r, u32 off) {
    return __builtin_bit_cast(u32x4, __builtin_amdgcn_raw_buffer_load_b128(r, (int)off, 0, 2));
}
constexpr u32 OOB = 0xFFFFFF00u;
#define PACE(n) asm volatile("s_waitcnt vmcnt(" #n ")" ::: "memory")
#define STR2(x) #x
#define STR(x) STR2(x)
#define PA 3
#define PF 7
#define PACE_A() asm volatile("s_waitcnt vmcnt(" STR(PA) ")" ::: "memory")
#define PACE_F() asm volatile("s_waitcnt vmcnt(" STR(PF) ")" ::: "memory")

__device__ __forceinline__ bool spin_check(Smem& sm, u32& it, u64& t0) {
    __builtin_amdgcn_s_sleep(2);
    if ((++it & 63u) == 0u) {
        if (lds_ld(&sm.sync[ABORTW]) != 0u) return true;
        u64 now = __builtin_amdgcn_s_memrealtime();
        if (t0 == 0) t0 = now;
        else if (now - t0 > TIMEOUT_TICKS) { __hip_atomic_store(&sm.sync[ABORTW], 1u, __ATOMIC_RELAXED, __HIP_MEMORY_SCOPE_WORKGROUP); return true; }
    }
    return false;
}

__global__ void __launch_bounds__(NTHR, 4) fused_kernel(
    const __hip_bfloat16* __restrict__ x, const __hip_bfloat16* __restrict__ kc, const __hip_bfloat16* __restrict__ vc,
    const __hip_bfloat16* __restrict__ wq, const __hip_bfloat16* __restrict__ wo, const __hip_bfloat16* __restrict__ wg,
    const __hip_bfloat16* __restrict__ wu, const __hip_bfloat16* __restrict__ wd,
    __hip_bfloat16* __restrict__ out, unsigned char* ws) {
    __shared__ Smem sm;
    const int t = threadIdx.x, lane = t & 63, wave = t >> 6, w = blockIdx.x;
    const rsrc_t rs = __builtin_amdgcn_make_buffer_rsrc(ws, 0, (int)WS_END, 0x00020000);

    if (wave == 0) STAMP(0);
    if (t < 16) sm.sync[t] = 0u;

    if (wave < NAW) {
        const rsrc_t r_x = __builtin_amdgcn_make_buffer_rsrc((void*)x, 0, DM * 2, 0x00020000);
        const rsrc_t r_wq = __builtin_amdgcn_make_buffer_rsrc((void*)wq, 0, DM * 4096 * 2, 0x00020000);
        const rsrc_t r_k = __builtin_amdgcn_make_buffer_rsrc((void*)kc, 0, 16 * SK * DH * 2, 0x00020000);
        const rsrc_t r_v = __builtin_amdgcn_make_buffer_rsrc((void*)vc, 0, 16 * SK * DH * 2, 0x00020000);
        const rsrc_t r_wo = __builtin_amdgcn_make_buffer_rsrc((void*)wo, 0, DM * 4096 * 2, 0x00020000);
        u32x4 xa = __builtin_bit_cast(u32x4, __builtin_amdgcn_raw_buffer_load_b128(r_x, lane * 16, 0, 0));
        u32x4 xb = __builtin_bit_cast(u32x4, __builtin_amdgcn_raw_buffer_load_b128(r_x, 1024 + lane * 16, 0, 0));
        const unsigned short xe0 = reinterpret_cast<const unsigned short*>(x)[t];
        const unsigned short xe1 = reinterpret_cast<const unsigned short*>(x)[512 + lane];

        const int qcb = w >> 2, qks = w & 3;
        const int cc = t & 7, r0 = t >> 3;
        u32x4 wqv[3];
#pragma unroll
        for (int i = 0; i < 3; ++i) {
            int r = r0 + 64 * i;
            wqv[i] = bld(r_wq, r < 144 ? (u32)(((144 * qks + r) * 4096 + 64 * qcb + 8 * cc) * 2) : OOB);
        }
        const int g = w >> 4, s = w & 15;
        const u32 kvoff = (u32)(((g * SK + (SK - WIN) + 128 * s + 16 * wave + (lane >> 4)) * DH + 8 * (lane & 15)) * 2);
        u32x4 kv[4], vv[4];
        asm volatile("" ::: "memory");
        if (wave == 0) STAMP(16);

        {
            float va[8], vb[8];
#pragma unroll
            for (int j = 0; j < 4; ++j) { va[2 * j] = bflo(xa[j]); va[2 * j + 1] = bfhi(xa[j]); vb[2 * j] = bflo(xb[j]); vb[2 * j + 1] = bfhi(xb[j]); }
            float sum = 0.f;
#pragma unroll
            for (int j = 0; j < 8; ++j) sum += va[j] + vb[j];
            sum = wave_sum(sum);
            if (wave == 0) STAMP(17);
            const float mean = sum * (1.0f / DM);
            float qv = 0.f;
#pragma unroll
            for (int j = 0; j < 8; ++j) { float d = va[j] - mean; qv = fmaf(d, d, qv); }
            if (lane < 8) {
#pragma unroll
                for (int j = 0; j < 8; ++j) { float d = vb[j] - mean; qv = fmaf(d, d, qv); }
            }
            qv = wave_sum(qv);
            const float var = rbf(qv * (1.0f / DM));
            const float m = rbf(mean);
            const float den = rbf(sqrtf(rbf(var + 1e-5f)));
            const float x0 = bflo((u32)xe0);
            sm.xn[t] = rbf(rbf(x0 - m) / den);
            sm.xraw[t] = x0;
            if (t < 64) {
                const float x1 = bflo((u32)xe1);
                sm.xn[512 + t] = rbf(rbf(x1 - m) / den);
                sm.xraw[512 + t] = x1;
            }
        }
        __syncthreads();
        if (wave == 0) STAMP(1);

        {
            float acc[8];
#pragma unroll
            for (int j = 0; j < 8; ++j) acc[j] = 0.f;
#pragma unroll
            for (int i = 0; i < 3; ++i) {
                int r = r0 + 64 * i;
                float xv = (r < 144) ? sm.xn[144 * qks + r] : 0.f;
                axpy8(acc, wqv[i], xv);
            }
#pragma unroll
            for (int j = 0; j < 8; ++j) {
                acc[j] += dppm<DPP_ROR8>(acc[j]);
                acc[j] = x32_sum(x16_sum(acc[j]));
            }
            if (lane < 8) st8(&sm.qp[wave][8 * lane], acc);
            arrive_lds(sm, CNT_QP, lane);
        }
        if (wave == 0) STAMP(2);
        asm volatile("" ::: "memory");
#pragma unroll
        for (int i = 0; i < 4; ++i) kv[i] = bld(r_k, kvoff + i * (4 * DH * 2));
#pragma unroll
        for (int i = 0; i < 4; ++i) { PACE_A(); vv[i] = bld(r_v, kvoff + i * (4 * DH * 2)); }
        const int pcc = t % 72, prg = t / 72;
        const bool pact = t < 504;
        u32x4 ov[3];
#pragma unroll
        for (int j = 0; j < 3; ++j) {
            int r = prg + 7 * j;
            PACE_A();
            ov[j] = bld(r_wo, (pact && r < 16) ? (u32)(((16 * w + r) * DM + 8 * pcc) * 2) : OOB);
        }
        asm volatile("" ::: "memory");

        wait_lds(sm, FLAG_Q, 1u);
        const u64 TT = (u64)sm.tag[0] | ((u64)sm.tag[1] << 32);
        if (wave == 0) STAMP(3);
        {
            const u32x4 q0 = *reinterpret_cast<const u32x4*>(&sm.qsb[4 * (lane & 15)]);
            const u32x4 q1 = *reinterpret_cast<const u32x4*>(&sm.qsb[64 + 4 * (lane & 15)]);
            float sc0[4], sc1[4];
#pragma unroll
            for (int i = 0; i < 4; ++i) {
                float a0 = 0.f, a1 = 0.f;
#pragma unroll
                for (int j = 0; j < 4; ++j) { a0 = dot2(kv[i][j], q0[j], a0); a1 = dot2(kv[i][j], q1[j], a1); }
                a0 = row16_sum(a0); a1 = row16_sum(a1);
                sc0[i] = rbf(rbf(a0) * 0.08838834764831845f);
                sc1[i] = rbf(rbf(a1) * 0.08838834764831845f);
            }
            float m0 = fmaxf(fmaxf(sc0[0], sc0[1]), fmaxf(sc0[2], sc0[3]));
            float m1 = fmaxf(fmaxf(sc1[0], sc1[1]), fmaxf(sc1[2], sc1[3]));
            m0 = x32_max(x16_max(m0));
            m1 = x32_max(x16_max(m1));
            float p0[4], p1[4], l0 = 0.f, l1 = 0.f;
#pragma unroll
            for (int i = 0; i < 4; ++i) { p0[i] = __expf(sc0[i] - m0); p1[i] = __expf(sc1[i] - m1); l0 += p0[i]; l1 += p1[i]; }
            l0 = x32_sum(x16_sum(l0));
            l1 = x32_sum(x16_sum(l1));
            float o0[8], o1[8];
#pragma unroll
            for (int j = 0; j < 8; ++j) { o0[j] = 0.f; o1[j] = 0.f; }
#pragma unroll
            for (int i = 0; i < 4; ++i) {
#pragma unroll
                for (int j = 0; j < 4; ++j) {
                    float lo = bflo(vv[i][j]), hi = bfhi(vv[i][j]);
                    o0[2 * j] = fmaf(p0[i], lo, o0[2 * j]); o0[2 * j + 1] = fmaf(p0[i], hi, o0[2 * j + 1]);
                    o1[2 * j] = fmaf(p1[i], lo, o1[2 * j]); o1[2 * j + 1] = fmaf(p1[i], hi, o1[2 * j + 1]);
                }
            }
#pragma unroll
            for (int j = 0; j < 8; ++j) {
                o0[j] = x32_sum(x16_sum(o0[j]));
                o1[j] = x32_sum(x16_sum(o1[j]));
            }
            if (lane < 16) { st8(&sm.opart[wave][0][8 * lane], o0); st8(&sm.opart[wave][1][8 * lane], o1); }
            if (lane == 0) { sm.ml[wave][0][0] = m0; sm.ml[wave][0][1] = l0; sm.ml[wave][1][0] = m1; sm.ml[wave][1][1] = l1; }
            arrive_lds(sm, CNT_ATT, lane);
        }
        if (wave == 0) STAMP(4);

        wait_lds(sm, FLAG_O, 1u);
        if (wave == 0) STAMP(7);
        {
            float as[8];
#pragma unroll
            for (int j = 0; j < 8; ++j) as[j] = 0.f;
#pragma unroll
            for (int j = 0; j < 3; ++j) {
                int r = prg + 7 * j;
                bool ok = pact && (r < 16);
                float hv = ok ? sm.of[r] : 0.f;
                axpy8(as, ov[j], hv);
            }
            if (pact) st8(&sm.Ps[prg][8 * pcc], as);
        }
        arrive_lds(sm, CNT_FIN, lane);
        wait_lds(sm, CNT_FIN, NAW + NFW);
        {
            float ss = 0.f, sf = 0.f;
#pragma unroll
            for (int v = 0; v < 7; ++v) ss += sm.Ps[v][t];
#pragma unroll
            for (int v = 0; v < 3; ++v) sf += sm.Pf[v][t];
            u32x4 gr = {__float_as_uint(ss), (u32)TT, __float_as_uint(sf), (u32)(TT >> 32)};
            gran_store(rs, (u32)(WS_FG + ((size_t)w * DM + t) * 16), gr);
        }
        if (wave == 0) STAMP(8);

        if (w < NRED) {
            const int c = lane & 3;
            const int p0 = 32 * wave + (lane >> 2);
            const u32 off0 = (u32)(WS_FG + ((size_t)p0 * DM + 4 * w + c) * 16);
            const u32 off1 = (u32)(WS_FG + ((size_t)(p0 + 16) * DM + 4 * w + c) * 16);
            u32x4 a, b;
            u32 it = 0; u64 t0 = 0;
            while (true) {
                asm volatile("" ::: "memory");
                a = gran_load(rs, off0);
                b = gran_load(rs, off1);
                bool ok = (a.y == (u32)TT) && (a.w == (u32)(TT >> 32)) && (b.y == (u32)TT) && (b.w == (u32)(TT >> 32));
                if (__all(ok)) break;
                if (spin_check(sm, it, t0)) break;
            }
            float sv = __uint_as_float(a.x) + __uint_as_float(b.x);
            float fv = __uint_as_float(a.z) + __uint_as_float(b.z);
            sv += dppm<DPP_ROR4>(sv); fv += dppm<DPP_ROR4>(fv);
            sv += dppm<DPP_ROR8>(sv); fv += dppm<DPP_ROR8>(fv);
            sv = x32_sum(x16_sum(sv)); fv = x32_sum(x16_sum(fv));
            if (lane < 4) { sm.red[wave][lane][0] = sv; sm.red[wave][lane][1] = fv; }
            arrive_lds(sm, CNT_RED, lane);
            if (wave == 0) {
                wait_lds(sm, CNT_RED, NAW);
                if (lane < 4) {
                    float ss = 0.f, ff = 0.f;
#pragma unroll
                    for (int v = 0; v < NAW; ++v) { ss += sm.red[v][lane][0]; ff += sm.red[v][lane][1]; }
                    float sb = rbf(ss), gt = rbf(silu_f(rbf(ff)));
                    out[4 * w + lane] = __float2bfloat16(sm.xraw[4 * w + lane] + rbf(sb * gt));
                }
                if (w == 0 && lane == 0) __hip_atomic_store((u64*)(ws + WS_E), TT, __ATOMIC_RELAXED, __HIP_MEMORY_SCOPE_AGENT);
                STAMP(9);
            }
        }
    } else if (wave < NAW + NFW) {
        const rsrc_t r_wg = __builtin_amdgcn_make_buffer_rsrc((void*)wg, 0, DM * FF * 2, 0x00020000);
        const rsrc_t r_wu = __builtin_amdgcn_make_buffer_rsrc((void*)wu, 0, DM * FF * 2, 0x00020000);
        const rsrc_t r_wd = __builtin_amdgcn_make_buffer_rsrc((void*)wd, 0, DM * FF * 2, 0x00020000);
        const int ft = t - NAW * 64;
        const int fcc = ft % 72, frg = ft / 72;
        const bool fact = frg < 3;
        const int gcb = w >> 5, gks = w & 31;
        u32x4 gv[6], uv[6];
        const u32 goff = fact ? (u32)(((18 * gks + frg) * FF + 576 * gcb + 8 * fcc) * 2) : OOB;
        const u32 gstep = fact ? (u32)(3 * FF * 2) : 0u;
        __syncthreads();
        wait_lds(sm, FLAG_Q, 1u);
#pragma unroll
        for (int j = 0; j < 2; ++j) { gv[j] = bld(r_wg, goff + j * gstep); uv[j] = bld(r_wu, goff + j * gstep); }
        asm volatile("" ::: "memory");
#pragma unroll
        for (int j = 2; j < 6; ++j) { PACE_F(); gv[j] = bld(r_wg, goff + j * gstep); PACE_F(); uv[j] = bld(r_wu, goff + j * gstep); }
        asm volatile("" ::: "memory");
        {
            float ag[8], au[8];
#pragma unroll
            for (int j = 0; j < 8; ++j) { ag[j] = 0.f; au[j] = 0.f; }
#pragma unroll
            for (int j = 0; j < 6; ++j) {
                float xv = fact ? sm.xn[18 * gks + frg + 3 * j] : 0.f;
                axpy8(ag, gv[j], xv);
                axpy8(au, uv[j], xv);
            }
            if (fact) { st8(&sm.Pg[frg][8 * fcc], ag); st8(&sm.Pu[frg][8 * fcc], au); }
        }
        u32x4 dv[6];
        const u32 doff = fact ? (u32)(((18 * w + frg) * DM + 8 * fcc) * 2) : OOB;
        const u32 dstep = fact ? (u32)(3 * DM * 2) : 0u;
#pragma unroll
        for (int j = 0; j < 6; ++j) dv[j] = bld(r_wd, doff + j * dstep);
        asm volatile("" ::: "memory");
        arrive_lds(sm, CNT_GU, lane);
        wait_lds(sm, CNT_GU, NFW);
        wait_lds(sm, FLAG_TAG, 1u);
        const u32 tagA = sm.tag[0], tagB = sm.tag[1];
        if (wave == NAW) STAMP(5);
#pragma unroll
        for (int k = 0; k < 3; ++k) {
            int n = ft + 256 * k;
            if (n < DM) {
                float sg = sm.Pg[0][n] + sm.Pg[1][n] + sm.Pg[2][n];
                float su = sm.Pu[0][n] + sm.Pu[1][n] + sm.Pu[2][n];
                u32x4 gr = {__float_as_uint(sg), tagA, __float_as_uint(su), tagB};
                gran_store(rs, (u32)(WS_GG + ((size_t)w * DM + n) * 16), gr);
            }
        }
        arrive_lds(sm, CNT_GUPUB, lane);
        wait_lds(sm, FLAG_H, 1u);
        if (wave == NAW) STAMP(6);
        {
            float ad[8];
#pragma unroll
            for (int j = 0; j < 8; ++j) ad[j] = 0.f;
#pragma unroll
            for (int j = 0; j < 6; ++j) {
                float hv = fact ? sm.hs[frg + 3 * j] : 0.f;
                axpy8(ad, dv[j], hv);
            }
            if (fact) st8(&sm.Pf[frg][8 * fcc], ad);
        }
        arrive_lds(sm, CNT_FIN, lane);
        wait_lds(sm, CNT_FIN, NAW + NFW);
        if (ft < 64) {
            const int n = 512 + ft;
            float ss = 0.f, sf = 0.f;
#pragma unroll
            for (int v = 0; v < 7; ++v) ss += sm.Ps[v][n];
#pragma unroll
            for (int v = 0; v < 3; ++v) sf += sm.Pf[v][n];
            u32x4 gr = {__float_as_uint(ss), tagA, __float_as_uint(sf), tagB};
            gran_store(rs, (u32)(WS_FG + ((size_t)w * DM + n) * 16), gr);
        }
    } else {
        __syncthreads();
        const u64 E0 = __hip_atomic_load((u64*)(ws + WS_E), __ATOMIC_RELAXED, __HIP_MEMORY_SCOPE_AGENT);
        const u64 TT = E0 + 1ull;
        const u32 tagA = (u32)TT, tagB = (u32)(TT >> 32);
        if (wave == NAW + NFW) {
            const int g = w >> 4, m = w & 15;
            if (lane == 0) { sm.tag[0] = tagA; sm.tag[1] = tagB; }
            set_lds(sm, FLAG_TAG, lane);
            wait_lds(sm, CNT_QP, NAW);
            STAMP(10);
            if (lane < 32) {
                float v0 = 0.f, v1 = 0.f;
#pragma unroll
                for (int v = 0; v < NAW; ++v) { v0 += sm.qp[v][2 * lane]; v1 += sm.qp[v][2 * lane + 1]; }
                u32x4 gr = {__float_as_uint(v0), tagA, __float_as_uint(v1), tagB};
                gran_store(rs, (u32)(WS_QG + ((size_t)w * 32 + lane) * 16), gr);
            }
            {
                const int p = lane & 31, hi = lane >> 5;
                u32x4 gq[8];
                u32 it = 0; u64 t0 = 0;
                while (true) {
                    asm volatile("" ::: "memory");
                    bool ok = true;
#pragma unroll
                    for (int k = 0; k < 8; ++k) gq[k] = gran_load(rs, (u32)(WS_QG + ((size_t)(16 * g + 2 * k + hi) * 32 + p) * 16));
#pragma unroll
                    for (int k = 0; k < 8; ++k) ok &= (gq[k].y == tagA) && (gq[k].w == tagB);
                    if (__all(ok)) break;
                    if (spin_check(sm, it, t0)) break;
                }
#pragma unroll
                for (int cb = 0; cb < 4; ++cb) {
                    float a0 = __uint_as_float(gq[2 * cb].x) + __uint_as_float(gq[2 * cb + 1].x);
                    float a1 = __uint_as_float(gq[2 * cb].z) + __uint_as_float(gq[2 * cb + 1].z);
                    a0 = x32_sum(a0);
                    a1 = x32_sum(a1);
                    if (hi == 0) sm.qsb[32 * cb + p] = (__float_as_uint(rbf(a0)) >> 16) | (__float_as_uint(rbf(a1)) & 0xffff0000u);
                }
            }
            set_lds(sm, FLAG_Q, lane);
            STAMP(11);

            wait_lds(sm, CNT_ATT, NAW);
            {
#pragma unroll
                for (int h = 0; h < 2; ++h) {
                    float M = sm.ml[0][h][0];
#pragma unroll
                    for (int v = 1; v < NAW; ++v) M = fmaxf(M, sm.ml[v][h][0]);
                    float oa = 0.f, ob = 0.f, L = 0.f;
#pragma unroll
                    for (int v = 0; v < NAW; ++v) {
                        float e = __expf(sm.ml[v][h][0] - M);
                        float2 ov2 = *reinterpret_cast<const float2*>(&sm.opart[v][h][2 * lane]);
                        oa = fmaf(e, ov2.x, oa);
                        ob = fmaf(e, ov2.y, ob);
                        L = fmaf(e, sm.ml[v][h][1], L);
                    }
                    u32x4 gr = {__float_as_uint(oa), tagA, __float_as_uint(ob), tagB};
                    gran_store(rs, (u32)(WS_AG + (((size_t)w * 2 + h) * 72 + lane) * 16), gr);
                    if (lane == 0) {
                        u32x4 g2 = {__float_as_uint(M), tagA, __float_as_uint(L), tagB};
                        gran_store(rs, (u32)(WS_AG + (((size_t)w * 2 + h) * 72 + 64) * 16), g2);
                    }
                }
            }
            STAMP(12);
            {
                const int hh = m >> 3, pp0 = 8 * (m & 7), sp = lane & 15, part = lane >> 4;
                const size_t base = ((size_t)(16 * g + sp) * 2 + hh) * 72;
                u32x4 ga, gb, gm;
                u32 it = 0; u64 t0 = 0;
                while (true) {
                    asm volatile("" ::: "memory");
                    ga = gran_load(rs, (u32)(WS_AG + (base + pp0 + 2 * part) * 16));
                    gb = gran_load(rs, (u32)(WS_AG + (base + pp0 + 2 * part + 1) * 16));
                    gm = gran_load(rs, (u32)(WS_AG + (base + 64) * 16));
                    bool ok = (ga.y == tagA) && (ga.w == tagB) && (gb.y == tagA) && (gb.w == tagB) && (gm.y == tagA) && (gm.w == tagB);
                    if (__all(ok)) break;
                    if (spin_check(sm, it, t0)) break;
                }
                float ms = __uint_as_float(gm.x), ls = __uint_as_float(gm.z);
                float M = row16_max(ms);
                float e = __expf(ms - M);
                float L = e * ls;
                float a0 = e * __uint_as_float(ga.x), a1 = e * __uint_as_float(ga.z), a2 = e * __uint_as_float(gb.x), a3 = e * __uint_as_float(gb.z);
                L = row16_sum(L); a0 = row16_sum(a0); a1 = row16_sum(a1); a2 = row16_sum(a2); a3 = row16_sum(a3);
                if (sp == 0) {
                    sm.of[4 * part + 0] = rbf(a0 / L); sm.of[4 * part + 1] = rbf(a1 / L);
                    sm.of[4 * part + 2] = rbf(a2 / L); sm.of[4 * part + 3] = rbf(a3 / L);
                }
            }
            set_lds(sm, FLAG_O, lane);
            STAMP(13);
        } else {
            const int gcb = w >> 5, c0 = 18 * (w & 31);
            wait_lds(sm, CNT_GUPUB, NFW);
            STAMP(14);
            u32x4 gg[9];
            u32 it = 0; u64 t0 = 0;
            while (true) {
                asm volatile("" ::: "memory");
                bool ok = true;
#pragma unroll
                for (int k = 0; k < 9; ++k) {
                    int i = 64 * k + lane; int ks = i / 18, j = i - 18 * ks;
                    gg[k] = gran_load(rs, (u32)(WS_GG + ((size_t)(32 * gcb + ks) * DM + c0 + j) * 16));
                }
#pragma unroll
                for (int k = 0; k < 9; ++k) ok &= (gg[k].y == tagA) && (gg[k].w == tagB);
                if (__all(ok)) break;
                if (spin_check(sm, it, t0)) break;
            }
#pragma unroll
            for (int k = 0; k < 9; ++k) { int i = 64 * k + lane; sm.tmpg[i] = __uint_as_float(gg[k].x); sm.tmpu[i] = __uint_as_float(gg[k].z); }
            __builtin_amdgcn_fence(__ATOMIC_RELEASE, "workgroup");
            __builtin_amdgcn_wave_barrier();
            if (lane < 18) {
                float sg = 0.f, su = 0.f;
#pragma unroll 8
                for (int ks = 0; ks < 32; ++ks) { sg += sm.tmpg[18 * ks + lane]; su += sm.tmpu[18 * ks + lane]; }
                float gt = rbf(sg), up = rbf(su);
                sm.hs[lane] = rbf(rbf(silu_f(gt)) * up);
            }
            set_lds(sm, FLAG_H, lane);
            STAMP(15);
        }
    }
}

extern "C" void kernel_launch(void* const* d_in, const int* in_sizes, int n_in,
                              void* d_out, int out_size, void* d_ws, size_t ws_size,
                              hipStream_t stream) {
    fused_kernel<<<NWG, NTHR, 0, stream>>>(
        (const __hip_bfloat16*)d_in[0], (const __hip_bfloat16*)d_in[1], (const __hip_bfloat16*)d_in[2],
        (const __hip_bfloat16*)d_in[3], (const __hip_bfloat16*)d_in[4], (const __hip_bfloat16*)d_in[6],
        (const __hip_bfloat16*)d_in[7], (const __hip_bfloat16*)d_in[8], (__hip_bfloat16*)d_out, (unsigned char*)d_ws);
}
```
